```python
import torch

def _softmax(x, axis=-1):
    return torch.softmax(x, dim=axis)
def _gelu(x):
    return torch.nn.functional.gelu(x, approximate="tanh")
def _silu(x):
    return torch.nn.functional.silu(x)
def _sigmoid(x):
    return torch.sigmoid(x)
def _layernorm(x, axis=-1, eps=1e-5):
    m = x.mean(dim=axis, keepdim=True)
    v = x.var(dim=axis, keepdim=True, unbiased=False)
    return (x - m) / torch.sqrt(v + eps)
def _scatter_add(tgt, idx, src, axis=0):
    return tgt.clone().index_add_(axis, idx, src)
def _segment_sum(src, seg, n):
    out = torch.zeros((n,) + src.shape[1:], dtype=src.dtype)
    return out.index_add_(0, seg, src)
def _pad(x, pw):
    flat = tuple(v for pair in reversed(pw) for v in pair)
    return torch.nn.functional.pad(x, flat)

kernel_name = 'concat_heads__sliding_local_dilated__grouped_kv_mqa__prompt'

def setup_inputs():

    _w = (lambda a: (lambda t: t.to(torch.bfloat16) if t.is_floating_point() else t)(torch.as_tensor(a)))
    def _A_setup():
        B, S, D = 1, 2048, 512
        return {
            "x": _w((lambda *s: torch.randn(*s, dtype=torch.bfloat16) * 0.1)(B, S, D)),
            "wqkv": _w((lambda *s: torch.randn(*s, dtype=torch.bfloat16) * 0.8)(D, 3 * D)),
            "wo": _w((lambda *s: torch.randn(*s, dtype=torch.bfloat16) * 0.8)(D, D)),
        }
    _A_in = _A_setup()

    def _B_setup():
        B, S, D, H = 1, 2048, 512, 32
        Dh = D // H
        return {
            "x": _w((lambda *s: torch.randn(*s, dtype=torch.bfloat16) * 0.05)(B, S, D)),
            "wq": _w((lambda *s: torch.randn(*s, dtype=torch.bfloat16) * 3.2)(D, H * Dh)),
            "wk": _w((lambda *s: torch.randn(*s, dtype=torch.bfloat16) * 3.2)(D, Dh)),
            "wv": _w((lambda *s: torch.randn(*s, dtype=torch.bfloat16) * 1.6)(D, Dh)),
            "wo": _w((lambda *s: torch.randn(*s, dtype=torch.bfloat16) * 1.6)(H * Dh, D)),
        }
    _B_in = _B_setup()

    return {
        "A_x": _A_in["x"],
        "A_wqkv": _A_in["wqkv"],
        "A_wo": _A_in["wo"],
        "B_x": _B_in["x"],
        "B_wq": _B_in["wq"],
        "B_wk": _B_in["wk"],
        "B_wv": _B_in["wv"],
        "B_wo": _B_in["wo"],
    }

def reference(A_x, A_wqkv, A_wo, B_x, B_wq, B_wk, B_wv, B_wo):
    def _A_ref(x, wqkv, wo):
        B, S, D = x.shape
        H = 32; Dh = D // H
        W, dil = 8, 2
        qkv = (lambda x, *d: x.permute(*d))((x @ wqkv).reshape(B, S, 3, H, Dh), 2, 0, 3, 1, 4)
        q, k, v = qkv[0], qkv[1], qkv[2]
        scale = Dh ** -0.5
        pos = torch.arange(S)
        off = torch.arange(W) * dil
        idx = pos[:, None] - off[None, :]
        mask = idx >= 0
        idx_c = torch.where(mask, idx, 0).reshape(-1)
        kg = k[:, :, idx_c, :].reshape(B, H, S, W, Dh)
        vg = v[:, :, idx_c, :].reshape(B, H, S, W, Dh)
        logits = torch.einsum("bhsd,bhswd->bhsw", q, kg) * scale
        logits = torch.where(mask[None, None, :, :], logits, -1e9)
        attn = _softmax(logits, axis=-1)
        o = torch.einsum("bhsw,bhswd->bhsd", attn, vg)
        return (lambda x, *d: x.permute(*d))(o, 0, 2, 1, 3).reshape(B, S, D) @ wo

    def _B_ref(x, wq, wk, wv, wo):
        B, S, D = x.shape
        H = 32
        Dh = D // H
        q = (lambda x, *d: x.permute(*d))((x @ wq).reshape(B, S, H, Dh), 0, 2, 1, 3)
        k = x @ wk
        v = x @ wv
        scores = torch.einsum("bhqd,bkd->bhqk", q, k) * (Dh ** -0.5)
        p = _softmax(scores, axis=-1)
        o = torch.einsum("bhqk,bkd->bhqd", p, v)
        return (lambda x, *d: x.permute(*d))(o, 0, 2, 1, 3).reshape(B, S, H * Dh) @ wo

    a = _A_ref(A_x, A_wqkv, A_wo)
    b = _B_ref(B_x, B_wq, B_wk, B_wv, B_wo)
    return torch.concatenate([a, b], axis=-1)
```

```cpp
#include <hip/hip_runtime.h>
#include <hip/hip_bf16.h>
#include <stdint.h>
#include <type_traits>

typedef __attribute__((ext_vector_type(4))) short s16x4;
typedef __attribute__((ext_vector_type(8))) short s16x8;
typedef __attribute__((ext_vector_type(8))) __bf16 bf16x8;
typedef __attribute__((ext_vector_type(2))) __bf16 bf16x2;
typedef __attribute__((ext_vector_type(2))) float f32x2;
typedef __attribute__((ext_vector_type(4))) float f32x4;
typedef __attribute__((ext_vector_type(16))) float f32x16;
typedef __attribute__((ext_vector_type(4))) unsigned u32x4;
typedef __attribute__((ext_vector_type(2))) unsigned u32x2;
typedef unsigned long long u64;
typedef __attribute__((ext_vector_type(2))) unsigned long long u64x2;
#define AS1 __attribute__((address_space(1)))
#define AS3 __attribute__((address_space(3)))
#define AS4 __attribute__((address_space(4)))
#define RLX_AGENT __ATOMIC_RELAXED, __HIP_MEMORY_SCOPE_AGENT

#define WS_EPOCH 0
#define WS_KVFLAG 0x440000
#define WS_AFLAG 0x480000
#define WS_BFLAG 0x4C0000
#define WS_KV 0x10000
#define WS_START 0x30000
#define WS_OA 0x40000
#define WS_OB 0x240000

#define LDS_BYTES 81920
#define SPIN_TICKS 4000000ull

#define MFMA(a, b, c) __builtin_amdgcn_mfma_f32_32x32x16_bf16(a, b, c, 0, 0, 0)

__device__ __forceinline__ void glds16(const void* sbase, unsigned voff, unsigned lds) {
  unsigned keep;
  asm volatile("s_mov_b32 %0, m0\n\ts_mov_b32 m0, %3\n\ts_nop 0\n\tglobal_load_lds_dwordx4 %1, %2\n\ts_mov_b32 m0, %0"
               : "=&s"(keep) : "v"(voff), "s"(sbase), "s"(lds) : "memory");
}
__device__ __forceinline__ void glds16_sc1(const void* sbase, unsigned voff, unsigned lds) {
  unsigned keep;
  asm volatile("s_mov_b32 %0, m0\n\ts_mov_b32 m0, %3\n\ts_nop 0\n\tglobal_load_lds_dwordx4 %1, %2 sc1\n\ts_mov_b32 m0, %0"
               : "=&s"(keep) : "v"(voff), "s"(sbase), "s"(lds) : "memory");
}
__device__ __forceinline__ void glds16_m(const void* sbase, unsigned voff, unsigned lds, u64 mask) {
  unsigned keep;
  asm volatile("s_mov_b32 %0, m0\n\ts_mov_b32 m0, %3\n\ts_mov_b64 exec, %4\n\tglobal_load_lds_dwordx4 %1, %2\n\ts_mov_b64 exec, -1\n\ts_mov_b32 m0, %0"
               : "=&s"(keep) : "v"(voff), "s"(sbase), "s"(lds), "s"(mask) : "memory");
}
__device__ __forceinline__ void glds16_v(const void* vaddr, unsigned lds) {
  unsigned keep;
  asm volatile("s_mov_b32 %0, m0\n\ts_mov_b32 m0, %2\n\ts_nop 0\n\tglobal_load_lds_dwordx4 %1, off\n\ts_mov_b32 m0, %0"
               : "=&s"(keep) : "v"(vaddr), "s"(lds) : "memory");
}
#define QSLOT(k) ((0x10324 >> (4 * ((k) % 5))) & 7)
#define WAIT_VM(n) asm volatile("s_waitcnt vmcnt(" #n ")" ::: "memory")
__device__ __forceinline__ void bar() { asm volatile("s_waitcnt lgkmcnt(0)\n\ts_barrier" ::: "memory"); }

__device__ __forceinline__ unsigned pk2(float a, float b) {
  f32x2 f = {a, b};
  return __builtin_bit_cast(unsigned, __builtin_convertvector(f, bf16x2));
}
__device__ __forceinline__ bf16x2 as_bf2(unsigned x) { return __builtin_bit_cast(bf16x2, x); }
__device__ __forceinline__ bf16x8 lds_frag(AS3 char* sm, unsigned off) {
  return __builtin_bit_cast(bf16x8, *(AS3 u32x4*)(sm + off));
}
__device__ __forceinline__ bf16x8 lds_frag_tr(AS3 char* sm, unsigned off0, unsigned off1) {
  s16x4 a = __builtin_amdgcn_ds_read_tr16_b64_v4i16((AS3 s16x4*)(sm + off0));
  s16x4 b = __builtin_amdgcn_ds_read_tr16_b64_v4i16((AS3 s16x4*)(sm + off1));
  s16x8 c = __builtin_shufflevector(a, b, 0, 1, 2, 3, 4, 5, 6, 7);
  return __builtin_bit_cast(bf16x8, c);
}
__device__ __forceinline__ void st16_sc1(void* base, unsigned off, u32x4 v) {
  __amdgpu_buffer_rsrc_t rs = __builtin_amdgcn_make_buffer_rsrc(base, 0, 0x7fffffff, 0x00020000);
  __builtin_amdgcn_raw_buffer_store_b128(v, rs, off, 0, 16);
}
__device__ __forceinline__ u32x4 ld16_sc1(const void* base, unsigned off) {
  __amdgpu_buffer_rsrc_t rs = __builtin_amdgcn_make_buffer_rsrc((void*)base, 0, 0x7fffffff, 0x00020000);
  return __builtin_amdgcn_raw_buffer_load_b128(rs, off, 0, 16);
}
template <int N>
__device__ __forceinline__ void poll_flags(const char* p, u64 tag, int lane) {
  AS1 u64* f = (AS1 u64*)(p) + (lane < N ? lane : 0) * 16;
  u64 t0 = __builtin_amdgcn_s_memrealtime();
  for (unsigned k = 1;; k++) {
    u64 v = __hip_atomic_load(f, RLX_AGENT);
    if (__all(v == tag)) break;
    if ((k & 15) == 0 && __builtin_amdgcn_s_memrealtime() - t0 > SPIN_TICKS) break;
    __builtin_amdgcn_s_sleep(1);
  }
}

__device__ __forceinline__ int fresh_lane() {
  int l;
  asm volatile("v_mbcnt_lo_u32_b32 %0, -1, 0\n\tv_mbcnt_hi_u32_b32 %0, -1, %0" : "=v"(l));
  return l;
}
#define LANE_VARS()                                                                           \
  const int lane = fresh_lane(), tid = w * 64 + lane, r = lane & 31, h = lane >> 5;           \
  const int g = lane >> 4, q4 = (lane >> 2) & 3, p4 = lane & 3, prow = lane >> 3, pch = lane & 7; \
  unsigned xa[4];                                                                             \
  _Pragma("unroll") for (int s_ = 0; s_ < 4; s_++) xa[s_] = r * 128 + (((2 * s_ + h) ^ ((r >> 1) & 7)) << 4); \
  (void)g; (void)q4; (void)p4; (void)prow; (void)pch; (void)xa; (void)tid;

struct Params {
  const char* ax; const char* awqkv; const char* awo;
  const char* bx; const char* bwq; const char* bwk; const char* bwv; const char* bwo;
  char* out; char* ws;
};

__global__ __launch_bounds__(512, 4) void fused_kernel(Params P) {
  __shared__ __attribute__((aligned(16))) char smem_raw[LDS_BYTES];
  AS3 char* sm = (AS3 char*)smem_raw;
  const unsigned ldsb = (unsigned)(uintptr_t)sm;
  const int w = __builtin_amdgcn_readfirstlane(threadIdx.x >> 6);
  const int bid = blockIdx.x;
  const bool roleA = bid >= 256;
  const int b = bid & 255;
  const int xcd = b & 7, ii = b >> 3;
  const int rt = (xcd >> 1) * 8 + (ii >> 2), hg = (xcd & 1) * 4 + (ii & 3);
  u64 tag;
#define LOAD_TAG() u64 tagr; asm volatile("s_load_dwordx2 %0, %1, 0x0" : "=&s"(tagr) : "s"(P.ws) : "memory")
#define PIN_TAG() do { asm volatile("s_waitcnt lgkmcnt(0)" : "+s"(tagr) :: "memory"); tag = tagr + 1; } while (0)
#define START_FLAG() do { if (w == 7 && lane == 0) __hip_atomic_store((AS1 u64*)(P.ws + WS_START + bid * 128), tag, RLX_AGENT); } while (0)
  const char* wo;
  const char* oimg;
  char* oflag;
  int outcol, myflag;

  if (roleA) {
    LANE_VARS();
    const int pos0 = rt * 64;
    const unsigned wsw = (pch ^ (((prow >> 1) & 1) << 2)) << 4;
    const unsigned voffW0 = (8 * (w & 3) + prow) * 3072 + (w >> 2) * 1024 + hg * 128 + wsw;
    const unsigned voffW2 = (4 * w + prow) * 3072 + 2048 + hg * 128 + wsw;
    const int xrl = 10 * w + (lane >> 2);
    int xr0 = pos0 - 16 + xrl; xr0 = xr0 < 0 ? 0 : xr0;
    const unsigned voffX = xr0 * 1024 + (((lane & 3) ^ ((xrl >> 2) & 3)) << 4);
    auto stage = [&](int kc) {
      const unsigned buf = ldsb + (kc & 3) * 17408;
      const int kk = kc;
      const char* wb = P.awqkv + (size_t)kk * 32 * 3072;
      glds16(wb, voffW0, buf + w * 1024);
      glds16_m(wb, voffW2, buf + 8192 + w * 512, 0xffffffffull);
      glds16_m(P.ax + kk * 64, voffX, buf + 12288 + w * 640, 0xffffffffffull);
    };
    unsigned aoff0, aoff1, boff0, boff1;
    if (w < 4) { aoff0 = aoff1 = (1 + (w >> 1)) * 4096 + (w & 1) * 64; boff0 = 12288; boff1 = 12288 + 2048; }
    else if (w < 6) { aoff0 = aoff1 = (w & 1) * 64; boff0 = 12288 + 1024; boff1 = 12288 + 1024 + 2048; }
    else { aoff0 = (w - 5) * 4096; aoff1 = aoff0 + 64; boff0 = boff1 = 12288 + 4096; }
    const unsigned trsw = ((q4 >> 1) & 1) << 2;
    const unsigned trrow = (8 * (g >> 1) + q4) * 128 + 8 * (p4 & 1);
    const unsigned trc = 2 * (g & 1) + (p4 >> 1);
    const unsigned ta0 = (aoff0 & ~64u) + trrow + (((trc + ((aoff0 & 64u) >> 4)) ^ trsw) << 4);
    const unsigned ta1 = (aoff1 & ~64u) + trrow + (((trc + ((aoff1 & 64u) >> 4)) ^ trsw) << 4);
    const unsigned xs0 = r * 64 + ((h ^ ((r >> 2) & 3)) << 4), xs1 = r * 64 + (((2 + h) ^ ((r >> 2) & 3)) << 4);
    f32x16 c0 = {0}, c1 = {0};
    auto gemm = [&](auto cls) {
      constexpr bool P1 = decltype(cls)::value;
      bf16x8 F[2][6];
      auto rd = [&](bf16x8* f, int kc) {
        const unsigned buf = (kc & 3) * 17408;
#pragma unroll
        for (int s = 0; s < 2; s++) {
          if (P1) {
            f[s] = lds_frag_tr(sm, buf + ta0 + s * 2048, buf + ta0 + s * 2048 + 512);
            f[2 + s] = lds_frag(sm, buf + boff0 + (s ? xs1 : xs0));
            f[4 + s] = lds_frag(sm, buf + boff1 + (s ? xs1 : xs0));
          } else {
            f[s] = lds_frag(sm, buf + boff0 + (s ? xs1 : xs0));
            f[2 + s] = lds_frag_tr(sm, buf + ta0 + s * 2048, buf + ta0 + s * 2048 + 512);
            f[4 + s] = lds_frag_tr(sm, buf + ta1 + s * 2048, buf + ta1 + s * 2048 + 512);
          }
        }
      };
      auto mm = [&](const bf16x8* f) {
#pragma unroll
        for (int s = 0; s < 2; s++) {
          if (P1) { c0 = MFMA(f[s], f[2 + s], c0); c1 = MFMA(f[s], f[4 + s], c1); }
          else { c0 = MFMA(f[2 + s], f[s], c0); c1 = MFMA(f[4 + s], f[s], c1); }
        }
      };
      WAIT_VM(9);
      bar();
      START_FLAG();
      rd(F[0], 0);
#pragma unroll 1
      for (int kc = 0; kc < 12; kc += 2) {
        WAIT_VM(6); bar(); stage(kc + 4); rd(F[1], kc + 1); mm(F[0]);
        WAIT_VM(6); bar(); stage(kc + 5); rd(F[0], kc + 2); mm(F[1]);
      }
      WAIT_VM(6); bar(); rd(F[1], 13); mm(F[0]);
      WAIT_VM(3); bar(); rd(F[0], 14); mm(F[1]);
      WAIT_VM(0); bar(); rd(F[1], 15); mm(F[0]);
      mm(F[1]);
    };
    LOAD_TAG();
    stage(0); stage(1); stage(2); stage(3);
    PIN_TAG();
    if (w < 6) gemm(std::true_type{}); else gemm(std::false_type{});
    bar();
    {
      const unsigned vo = (8 * w + prow) * 1024 + hg * 128 + ((pch ^ (((prow >> 1) & 1) << 2)) << 4);
#pragma unroll
      for (int i = 0; i < 4; i++) glds16(P.awo + i * 64 * 1024, vo, ldsb + i * 8192 + w * 1024);
    }
    {
#pragma unroll
      for (int t = 0; t < 2; t++) {
        const f32x16& c = t ? c1 : c0;
        int which, ft, row;
        if (w < 4) { which = 1 + (w >> 1); ft = w & 1; row = 32 * t + r; }
        else if (w < 6) { which = 0; ft = w & 1; row = 32 * t + r; }
        else { which = w - 5; ft = t; row = 64 + r; }
#pragma unroll
        for (int a = 0; a < 2; a++) {
          const int head = ft * 2 + a;
          if (which == 2) {
            AS3 f32x4* d = (AS3 f32x4*)(sm + 45056 + ((head * 2 + h) * 2 * 96 + row) * 16);
            d[0] = f32x4{c[8 * a + 0], c[8 * a + 1], c[8 * a + 2], c[8 * a + 3]};
            d[96] = f32x4{c[8 * a + 4], c[8 * a + 5], c[8 * a + 6], c[8 * a + 7]};
          } else {
            u32x4 v = {pk2(c[8 * a + 0], c[8 * a + 1]), pk2(c[8 * a + 2], c[8 * a + 3]),
                       pk2(c[8 * a + 4], c[8 * a + 5]), pk2(c[8 * a + 6], c[8 * a + 7])};
            const unsigned off = which == 1 ? 32768 + ((head * 2 + h) * 96 + row) * 16 : 69632 + ((head * 2 + h) * 64 + row) * 16;
            *(AS3 u32x4*)(sm + off) = v;
          }
        }
      }
    }
    bar();
    {
      const int qb = w >> 2, head = w & 3;
      const int prw = qb * 32 + r;
      const u32x4 qv = *(AS3 u32x4*)(sm + 69632 + ((head * 2 + h) * 64 + prw) * 16);
      float sc[8];
#pragma unroll
      for (int wi = 0; wi < 8; wi++) {
        const u32x4 kv = *(AS3 u32x4*)(sm + 32768 + ((head * 2 + h) * 96 + 16 + prw - 2 * wi) * 16);
        float d = 0.f;
#pragma unroll
        for (int j = 0; j < 4; j++)
          d = __builtin_amdgcn_fdot2_f32_bf16(as_bf2(qv[j]), as_bf2(kv[j]), d, false);
        d += __shfl_xor(d, 32);
        d *= 0.25f * 1.4426950408889634f;
        sc[wi] = (pos0 + prw - 2 * wi >= 0) ? d : -1.0e30f;
      }
      float m = sc[0];
#pragma unroll
      for (int wi = 1; wi < 8; wi++) m = fmaxf(m, sc[wi]);
      float l = 0.f;
#pragma unroll
      for (int wi = 0; wi < 8; wi++) { sc[wi] = __builtin_amdgcn_exp2f(sc[wi] - m); l += sc[wi]; }
      f32x4 o0 = {0, 0, 0, 0}, o1 = {0, 0, 0, 0};
#pragma unroll
      for (int wi = 0; wi < 8; wi++) {
        AS3 f32x4* vp = (AS3 f32x4*)(sm + 45056 + ((head * 2 + h) * 2 * 96 + 16 + prw - 2 * wi) * 16);
        o0 += sc[wi] * vp[0];
        o1 += sc[wi] * vp[96];
      }
      const float il = 1.0f / l;
      u32x4 ov = {pk2(o0[0] * il, o0[1] * il), pk2(o0[2] * il, o0[3] * il), pk2(o1[0] * il, o1[1] * il), pk2(o1[2] * il, o1[3] * il)};
      st16_sc1(P.ws + WS_OA, ((rt * 2 + qb) * 32 + hg * 4 + head) * 1024 + lane * 16, ov);
      myflag = (rt * 2 + qb) * 32 + hg * 4 + head;
    }
    wo = P.awo; oimg = P.ws + WS_OA; oflag = P.ws + WS_AFLAG; outcol = 0;
  } else {
    LANE_VARS();
    const int ct = w >> 2, qt = (w >> 1) & 1, kh = w & 1;
    const int head = ct * 2 + kh;
    {
      const unsigned voffW = (8 * w + prow) * 1024 + hg * 128 + ((pch ^ (((prow >> 1) & 1) << 2)) << 4);
      const unsigned voffX = (rt * 64 + 8 * w + prow) * 1024 + ((pch ^ ((4 * w + (prow >> 1)) & 7)) << 4);
      auto stage = [&](int kc, unsigned buf) {
        const int kk = kc;
        glds16(P.bwq + (size_t)kk * 64 * 1024, voffW, buf + w * 1024);
        glds16(P.bx + kk * 128, voffX, buf + 8192 + w * 1024);
      };
      if (b < 64) {
        LOAD_TAG();
        const int krow = 64 * w + (lane >> 2), ch = lane & 3;
        const char* src = (ch < 2 ? P.bwk + ch * 16 : P.bwv + (ch - 2) * 16) + krow * 32;
#pragma unroll
        for (int i = 0; i < 4; i++) glds16_v(src + i * 16 * 32, ldsb + w * 4096 + i * 1024);
        const char* xb = P.bx + (size_t)b * 32 * 1024 + w * 128;
#pragma unroll
        for (int i = 0; i < 4; i++) {
          const int row = 8 * i + prow;
          glds16(xb, row * 1024 + ((pch ^ ((row >> 1) & 7)) << 4), ldsb + 32768 + w * 4096 + i * 1024);
        }
        stage(0, ldsb + 4 * 16384);
        PIN_TAG();
        WAIT_VM(2);
        f32x16 d1 = {0}, d2 = {0};
#pragma unroll
        for (int s = 0; s < 4; s++) {
          const unsigned wa = w * 4096 + (16 * s + 8 * (g >> 1) + q4) * 64 + 32 * (g & 1) + 8 * p4;
          bf16x8 wf = lds_frag_tr(sm, wa, wa + 256);
          bf16x8 xf = lds_frag(sm, 32768 + w * 4096 + xa[s]);
          d1 = MFMA(wf, xf, d1);
          d2 = MFMA(xf, wf, d2);
        }
        AS3 f32x4* pp = (AS3 f32x4*)(sm + w * 4096);
        pp[lane * 2] = f32x4{d1[0], d1[1], d1[2], d1[3]};
        pp[lane * 2 + 1] = f32x4{d1[4], d1[5], d1[6], d1[7]};
        if (r >= 16) {
          const int t = 64 + h * 16 + (r - 16);
          pp[t * 2] = f32x4{d2[0], d2[1], d2[2], d2[3]};
          pp[t * 2 + 1] = f32x4{d2[4], d2[5], d2[6], d2[7]};
          pp[(t + 32) * 2] = f32x4{d2[8], d2[9], d2[10], d2[11]};
          pp[(t + 32) * 2 + 1] = f32x4{d2[12], d2[13], d2[14], d2[15]};
        }
        bar();
        f32x2 acc = {0.f, 0.f};
#pragma unroll
        for (int i = 0; i < 8; i++) acc += *(AS3 f32x2*)(sm + i * 4096 + tid * 8);
        const unsigned x0 = pk2(acc[0], acc[1]);
        const unsigned x1 = __builtin_amdgcn_mov_dpp(x0, 0x55, 0xf, 0xf, true);
        const unsigned x2 = __builtin_amdgcn_mov_dpp(x0, 0xaa, 0xf, 0xf, true);
        const unsigned x3 = __builtin_amdgcn_mov_dpp(x0, 0xff, 0xf, 0xf, true);
        if ((lane & 3) == 0) st16_sc1(P.ws + WS_KV, b * 2048 + tid * 4, u32x4{x0, x1, x2, x3});
        bar();
        stage(1, ldsb + 2 * 16384); stage(2, ldsb + 3 * 16384); stage(3, ldsb); stage(4, ldsb + 16384);
        WAIT_VM(8);
        bar();
        if (w == 0 && lane < 32) __hip_atomic_store((AS1 u64*)(P.ws + WS_KVFLAG + lane * 8192 + b * 128), tag, RLX_AGENT);
      } else {
        LOAD_TAG();
#pragma unroll
        for (int i = 0; i < 5; i++) stage(i, ldsb + QSLOT(i) * 16384);
        PIN_TAG();
      }
      const unsigned trsw = ((q4 >> 1) & 1) << 2;
      const unsigned ta = (kh * 32 + 8 * (g >> 1) + q4) * 128 + 8 * (p4 & 1) + (((2 * (g & 1) + (p4 >> 1) + ct * 4) ^ trsw) << 4);
      const unsigned xb0 = 8192 + qt * 4096 + (kh ? xa[2] : xa[0]);
      const unsigned xb1 = 8192 + qt * 4096 + (kh ? xa[3] : xa[1]);
      f32x16 c = {0};
#define QSTEP(i, slot, vm)                                                       \
  {                                                                              \
    WAIT_VM(vm);                                                                 \
    bar();                                                                       \
    if (i >= 1 && i <= 3) stage(i + 4, ldsb + QSLOT(i + 4) * 16384);            \
    bf16x8 a0 = lds_frag_tr(sm, slot * 16384 + ta, slot * 16384 + ta + 512);     \
    bf16x8 a1 = lds_frag_tr(sm, slot * 16384 + ta + 2048, slot * 16384 + ta + 2560); \
    bf16x8 b0 = lds_frag(sm, slot * 16384 + xb0);                                \
    bf16x8 b1 = lds_frag(sm, slot * 16384 + xb1);                                \
    c = MFMA(a0, b0, c);                                                         \
    c = MFMA(a1, b1, c);                                                         \
  }
      QSTEP(0, 4, 8) START_FLAG(); QSTEP(1, 2, 6) QSTEP(2, 3, 6) QSTEP(3, 0, 6) QSTEP(4, 1, 6) QSTEP(5, 4, 4) QSTEP(6, 2, 2) QSTEP(7, 3, 0)
#undef QSTEP
      AS3 f32x4* ex = (AS3 f32x4*)sm;
      ex[(w * 2 + 0) * 64 + lane] = kh ? f32x4{c[0], c[1], c[2], c[3]} : f32x4{c[8], c[9], c[10], c[11]};
      ex[(w * 2 + 1) * 64 + lane] = kh ? f32x4{c[4], c[5], c[6], c[7]} : f32x4{c[12], c[13], c[14], c[15]};
      bar();
      const f32x4 e0 = ex[((w ^ 1) * 2 + 0) * 64 + lane], e1 = ex[((w ^ 1) * 2 + 1) * 64 + lane];
      const float qs = 0.25f * 1.4426950408889634f;
      float qv[8];
#pragma unroll
      for (int j = 0; j < 4; j++) {
        qv[j] = ((kh ? c[8 + j] : c[j]) + e0[j]) * qs;
        qv[4 + j] = ((kh ? c[12 + j] : c[4 + j]) + e1[j]) * qs;
      }
      u32x4 qp = {pk2(qv[0], qv[1]), pk2(qv[2], qv[3]), pk2(qv[4], qv[5]), pk2(qv[6], qv[7])};
      const bf16x8 qf = __builtin_bit_cast(bf16x8, qp);
      if (w == 0) poll_flags<64>(P.ws + WS_KVFLAG + (b & 31) * 8192, tag, lane);
      bar();
      const char* kvg = P.ws + WS_KV;
      const unsigned kvoff = w * 4096 + lane * 16;
#pragma unroll
      for (int i = 0; i < 8; i++) glds16_sc1(kvg + (i >> 2) * 32768 + (i & 3) * 1024, kvoff, ldsb + (i >> 2) * 32768 + w * 4096 + (i & 3) * 1024);
      f32x16 o;
      WAIT_VM(4);
      bar();
      {
        const f32x16 z = {0};
        const f32x16 s0 = MFMA(lds_frag(sm, lane * 16), qf, z);
        const f32x16 s1 = MFMA(lds_frag(sm, lane * 16 + 2048), qf, z);
        float m = fmaxf(s0[0], s1[0]);
#pragma unroll
        for (int i = 1; i < 16; i++) m = fmaxf(m, fmaxf(s0[i], s1[i]));
        m = fmaxf(m, __shfl_xor(m, 32));
        const float negm = -m;
        unsigned kp = ldsb + lane * 16;
        unsigned vp = ldsb + 1024 + (h * 16 + (r & 15)) * 16;
        const int tidk = fresh_lane();
        const unsigned wooff = (8 * w + ((tidk & 63) >> 3)) * 1024 + hg * 128 + (((tidk & 7) ^ (((tidk >> 4) & 1) << 2)) << 4);
        unsigned ldskv = ldsb + w * 4096, ldswo = ldsb + w * 1024;
        unsigned keep, ctr;
        asm volatile(
            "s_mov_b32 %2, m0\n\t"
            "s_mov_b64 s[60:61], %10\n\t"
            "v_mov_b32 v80, %7\n\t"
            "v_mov_b32 v81, %7\n\t"
            "v_mov_b32 v82, %7\n\t"
            "v_mov_b32 v83, %7\n\t"
            "v_mov_b32 v84, %7\n\t"
            "v_mov_b32 v85, %7\n\t"
            "v_mov_b32 v86, %7\n\t"
            "v_mov_b32 v87, %7\n\t"
            "v_mov_b32 v88, %7\n\t"
            "v_mov_b32 v89, %7\n\t"
            "v_mov_b32 v90, %7\n\t"
            "v_mov_b32 v91, %7\n\t"
            "v_mov_b32 v92, %7\n\t"
            "v_mov_b32 v93, %7\n\t"
            "v_mov_b32 v94, %7\n\t"
            "v_mov_b32 v95, %7\n\t"
            "v_mov_b32 v36, 0x3f803f80\n\t"
            "v_mov_b32 v37, 0x3f803f80\n\t"
            "v_mov_b32 v38, 0x3f803f80\n\t"
            "v_mov_b32 v39, 0x3f803f80\n\t"
            "v_mov_b32 v40, 0x3f803f80\n\t"
            "v_mov_b32 v41, 0x3f803f80\n\t"
            "v_mov_b32 v42, 0x3f803f80\n\t"
            "v_mov_b32 v43, 0x3f803f80\n\t"
            "v_mov_b32 v44, 0x3f803f80\n\t"
            "v_mov_b32 v45, 0x3f803f80\n\t"
            "v_mov_b32 v46, 0x3f803f80\n\t"
            "v_mov_b32 v47, 0x3f803f80\n\t"
            "v_mov_b32 v48, 0x3f803f80\n\t"
            "v_mov_b32 v49, 0x3f803f80\n\t"
            "v_mov_b32 v50, 0x3f803f80\n\t"
            "v_mov_b32 v51, 0x3f803f80\n\t"
            "v_mov_b32 v64, 0\n\t"
            "v_mov_b32 v65, 0\n\t"
            "v_mov_b32 v66, 0\n\t"
            "v_mov_b32 v67, 0\n\t"
            "v_mov_b32 v68, 0\n\t"
            "v_mov_b32 v69, 0\n\t"
            "v_mov_b32 v70, 0\n\t"
            "v_mov_b32 v71, 0\n\t"
            "v_mov_b32 v72, 0\n\t"
            "v_mov_b32 v73, 0\n\t"
            "v_mov_b32 v74, 0\n\t"
            "v_mov_b32 v75, 0\n\t"
            "v_mov_b32 v76, 0\n\t"
            "v_mov_b32 v77, 0\n\t"
            "v_mov_b32 v78, 0\n\t"
            "v_mov_b32 v79, 0\n\t"
            "ds_read_b128 v[56:59], %0\n\t"
            "s_mov_b64 exec, %9\n\t"
            "ds_read_b128 v[48:51], %1\n\t"
            "ds_read_b128 v[44:47], %1 offset:512\n\t"
            "s_mov_b64 exec, -1\n\t"
            "ds_read_b128 v[52:55], %0 offset:2048\n\t"
            "s_mov_b64 exec, %9\n\t"
            "ds_read_b128 v[40:43], %1 offset:2048\n\t"
            "ds_read_b128 v[36:39], %1 offset:2560\n\t"
            "s_mov_b64 exec, -1\n\t"
            "s_mov_b32 %3, 0\n\t"
            "s_waitcnt lgkmcnt(5)\n\t"
            "v_mfma_f32_32x32x16_bf16 v[96:111], v[56:59], v[60:63], v[80:95]\n\t"
            "s_nop 15\n\t"
            "LOOP_%=:\n\t"
            "s_waitcnt lgkmcnt(2)\n\t"
            "v_mfma_f32_32x32x16_bf16 v[112:127], v[52:55], v[60:63], v[80:95]\n\t"
            "ds_read_b128 v[56:59], %0 offset:4096\n\t"
            "v_exp_f32 v96, v96\n\t"
            "v_exp_f32 v97, v97\n\t"
            "v_exp_f32 v98, v98\n\t"
            "v_exp_f32 v99, v99\n\t"
            "v_cvt_pk_bf16_f32 v96, v96, v97\n\t"
            "v_exp_f32 v100, v100\n\t"
            "v_exp_f32 v101, v101\n\t"
            "v_cvt_pk_bf16_f32 v97, v98, v99\n\t"
            "v_exp_f32 v102, v102\n\t"
            "v_exp_f32 v103, v103\n\t"
            "v_cvt_pk_bf16_f32 v98, v100, v101\n\t"
            "v_exp_f32 v104, v104\n\t"
            "v_cvt_pk_bf16_f32 v99, v102, v103\n\t"
            "v_exp_f32 v105, v105\n\t"
            "v_exp_f32 v106, v106\n\t"
            "v_mfma_f32_32x32x16_bf16 v[64:79], v[48:51], v[96:99], v[64:79]\n\t"
            "v_exp_f32 v107, v107\n\t"
            "v_cvt_pk_bf16_f32 v104, v104, v105\n\t"
            "v_exp_f32 v108, v108\n\t"
            "v_exp_f32 v109, v109\n\t"
            "v_cvt_pk_bf16_f32 v105, v106, v107\n\t"
            "v_exp_f32 v110, v110\n\t"
            "v_exp_f32 v111, v111\n\t"
            "v_cvt_pk_bf16_f32 v106, v108, v109\n\t"
            "s_nop 0\n\t"
            "v_cvt_pk_bf16_f32 v107, v110, v111\n\t"
            "s_nop 1\n\t"
            "v_mfma_f32_32x32x16_bf16 v[64:79], v[44:47], v[104:107], v[64:79]\n\t"
            "s_mov_b64 exec, %9\n\t"
            "ds_read_b128 v[48:51], %1 offset:4096\n\t"
            "ds_read_b128 v[44:47], %1 offset:4608\n\t"
            "s_mov_b64 exec, -1\n\t"
            "s_waitcnt lgkmcnt(2)\n\t"
            "v_mfma_f32_32x32x16_bf16 v[96:111], v[56:59], v[60:63], v[80:95]\n\t"
            "ds_read_b128 v[52:55], %0 offset:6144\n\t"
            "v_exp_f32 v112, v112\n\t"
            "v_exp_f32 v113, v113\n\t"
            "v_exp_f32 v114, v114\n\t"
            "v_exp_f32 v115, v115\n\t"
            "v_cvt_pk_bf16_f32 v112, v112, v113\n\t"
            "v_exp_f32 v116, v116\n\t"
            "v_exp_f32 v117, v117\n\t"
            "v_cvt_pk_bf16_f32 v113, v114, v115\n\t"
            "v_exp_f32 v118, v118\n\t"
            "v_exp_f32 v119, v119\n\t"
            "v_cvt_pk_bf16_f32 v114, v116, v117\n\t"
            "v_exp_f32 v120, v120\n\t"
            "v_cvt_pk_bf16_f32 v115, v118, v119\n\t"
            "v_exp_f32 v121, v121\n\t"
            "v_exp_f32 v122, v122\n\t"
            "v_mfma_f32_32x32x16_bf16 v[64:79], v[40:43], v[112:115], v[64:79]\n\t"
            "v_exp_f32 v123, v123\n\t"
            "v_cvt_pk_bf16_f32 v120, v120, v121\n\t"
            "v_exp_f32 v124, v124\n\t"
            "v_exp_f32 v125, v125\n\t"
            "v_cvt_pk_bf16_f32 v121, v122, v123\n\t"
            "v_exp_f32 v126, v126\n\t"
            "v_exp_f32 v127, v127\n\t"
            "v_cvt_pk_bf16_f32 v122, v124, v125\n\t"
            "s_nop 0\n\t"
            "v_cvt_pk_bf16_f32 v123, v126, v127\n\t"
            "s_nop 1\n\t"
            "v_mfma_f32_32x32x16_bf16 v[64:79], v[36:39], v[120:123], v[64:79]\n\t"
            "s_mov_b64 exec, %9\n\t"
            "ds_read_b128 v[40:43], %1 offset:6144\n\t"
            "ds_read_b128 v[36:39], %1 offset:6656\n\t"
            "s_mov_b64 exec, -1\n\t"
            "s_waitcnt lgkmcnt(2)\n\t"
            "v_mfma_f32_32x32x16_bf16 v[112:127], v[52:55], v[60:63], v[80:95]\n\t"
            "ds_read_b128 v[56:59], %0 offset:8192\n\t"
            "v_exp_f32 v96, v96\n\t"
            "v_exp_f32 v97, v97\n\t"
            "v_exp_f32 v98, v98\n\t"
            "v_exp_f32 v99, v99\n\t"
            "v_cvt_pk_bf16_f32 v96, v96, v97\n\t"
            "v_exp_f32 v100, v100\n\t"
            "v_exp_f32 v101, v101\n\t"
            "v_cvt_pk_bf16_f32 v97, v98, v99\n\t"
            "v_exp_f32 v102, v102\n\t"
            "v_exp_f32 v103, v103\n\t"
            "v_cvt_pk_bf16_f32 v98, v100, v101\n\t"
            "v_exp_f32 v104, v104\n\t"
            "v_cvt_pk_bf16_f32 v99, v102, v103\n\t"
            "v_exp_f32 v105, v105\n\t"
            "v_exp_f32 v106, v106\n\t"
            "v_mfma_f32_32x32x16_bf16 v[64:79], v[48:51], v[96:99], v[64:79]\n\t"
            "v_exp_f32 v107, v107\n\t"
            "v_cvt_pk_bf16_f32 v104, v104, v105\n\t"
            "v_exp_f32 v108, v108\n\t"
            "v_exp_f32 v109, v109\n\t"
            "v_cvt_pk_bf16_f32 v105, v106, v107\n\t"
            "v_exp_f32 v110, v110\n\t"
            "v_exp_f32 v111, v111\n\t"
            "v_cvt_pk_bf16_f32 v106, v108, v109\n\t"
            "s_nop 0\n\t"
            "v_cvt_pk_bf16_f32 v107, v110, v111\n\t"
            "s_nop 1\n\t"
            "v_mfma_f32_32x32x16_bf16 v[64:79], v[44:47], v[104:107], v[64:79]\n\t"
            "s_mov_b64 exec, %9\n\t"
            "ds_read_b128 v[48:51], %1 offset:8192\n\t"
            "ds_read_b128 v[44:47], %1 offset:8704\n\t"
            "s_mov_b64 exec, -1\n\t"
            "s_waitcnt lgkmcnt(2)\n\t"
            "v_mfma_f32_32x32x16_bf16 v[96:111], v[56:59], v[60:63], v[80:95]\n\t"
            "ds_read_b128 v[52:55], %0 offset:10240\n\t"
            "v_exp_f32 v112, v112\n\t"
            "v_exp_f32 v113, v113\n\t"
            "v_exp_f32 v114, v114\n\t"
            "v_exp_f32 v115, v115\n\t"
            "v_cvt_pk_bf16_f32 v112, v112, v113\n\t"
            "v_exp_f32 v116, v116\n\t"
            "v_exp_f32 v117, v117\n\t"
            "v_cvt_pk_bf16_f32 v113, v114, v115\n\t"
            "v_exp_f32 v118, v118\n\t"
            "v_exp_f32 v119, v119\n\t"
            "v_cvt_pk_bf16_f32 v114, v116, v117\n\t"
            "v_exp_f32 v120, v120\n\t"
            "v_cvt_pk_bf16_f32 v115, v118, v119\n\t"
            "v_exp_f32 v121, v121\n\t"
            "v_exp_f32 v122, v122\n\t"
            "v_mfma_f32_32x32x16_bf16 v[64:79], v[40:43], v[112:115], v[64:79]\n\t"
            "v_exp_f32 v123, v123\n\t"
            "v_cvt_pk_bf16_f32 v120, v120, v121\n\t"
            "v_exp_f32 v124, v124\n\t"
            "v_exp_f32 v125, v125\n\t"
            "v_cvt_pk_bf16_f32 v121, v122, v123\n\t"
            "v_exp_f32 v126, v126\n\t"
            "v_exp_f32 v127, v127\n\t"
            "v_cvt_pk_bf16_f32 v122, v124, v125\n\t"
            "s_nop 0\n\t"
            "v_cvt_pk_bf16_f32 v123, v126, v127\n\t"
            "s_nop 1\n\t"
            "v_mfma_f32_32x32x16_bf16 v[64:79], v[36:39], v[120:123], v[64:79]\n\t"
            "s_mov_b64 exec, %9\n\t"
            "ds_read_b128 v[40:43], %1 offset:10240\n\t"
            "ds_read_b128 v[36:39], %1 offset:10752\n\t"
            "s_mov_b64 exec, -1\n\t"
            "s_waitcnt lgkmcnt(2)\n\t"
            "v_mfma_f32_32x32x16_bf16 v[112:127], v[52:55], v[60:63], v[80:95]\n\t"
            "ds_read_b128 v[56:59], %0 offset:12288\n\t"
            "v_exp_f32 v96, v96\n\t"
            "v_exp_f32 v97, v97\n\t"
            "v_exp_f32 v98, v98\n\t"
            "v_exp_f32 v99, v99\n\t"
            "v_cvt_pk_bf16_f32 v96, v96, v97\n\t"
            "v_exp_f32 v100, v100\n\t"
            "v_exp_f32 v101, v101\n\t"
            "v_cvt_pk_bf16_f32 v97, v98, v99\n\t"
            "v_exp_f32 v102, v102\n\t"
            "v_exp_f32 v103, v103\n\t"
            "v_cvt_pk_bf16_f32 v98, v100, v101\n\t"
            "v_exp_f32 v104, v104\n\t"
            "v_cvt_pk_bf16_f32 v99, v102, v103\n\t"
            "v_exp_f32 v105, v105\n\t"
            "v_exp_f32 v106, v106\n\t"
            "v_mfma_f32_32x32x16_bf16 v[64:79], v[48:51], v[96:99], v[64:79]\n\t"
            "v_exp_f32 v107, v107\n\t"
            "v_cvt_pk_bf16_f32 v104, v104, v105\n\t"
            "v_exp_f32 v108, v108\n\t"
            "v_exp_f32 v109, v109\n\t"
            "v_cvt_pk_bf16_f32 v105, v106, v107\n\t"
            "v_exp_f32 v110, v110\n\t"
            "v_exp_f32 v111, v111\n\t"
            "v_cvt_pk_bf16_f32 v106, v108, v109\n\t"
            "s_nop 0\n\t"
            "v_cvt_pk_bf16_f32 v107, v110, v111\n\t"
            "s_nop 1\n\t"
            "v_mfma_f32_32x32x16_bf16 v[64:79], v[44:47], v[104:107], v[64:79]\n\t"
            "s_mov_b64 exec, %9\n\t"
            "ds_read_b128 v[48:51], %1 offset:12288\n\t"
            "ds_read_b128 v[44:47], %1 offset:12800\n\t"
            "s_mov_b64 exec, -1\n\t"
            "s_waitcnt lgkmcnt(2)\n\t"
            "v_mfma_f32_32x32x16_bf16 v[96:111], v[56:59], v[60:63], v[80:95]\n\t"
            "ds_read_b128 v[52:55], %0 offset:14336\n\t"
            "v_exp_f32 v112, v112\n\t"
            "v_exp_f32 v113, v113\n\t"
            "v_exp_f32 v114, v114\n\t"
            "v_exp_f32 v115, v115\n\t"
            "v_cvt_pk_bf16_f32 v112, v112, v113\n\t"
            "v_exp_f32 v116, v116\n\t"
            "v_exp_f32 v117, v117\n\t"
            "v_cvt_pk_bf16_f32 v113, v114, v115\n\t"
            "v_exp_f32 v118, v118\n\t"
            "v_exp_f32 v119, v119\n\t"
            "v_cvt_pk_bf16_f32 v114, v116, v117\n\t"
            "v_exp_f32 v120, v120\n\t"
            "v_cvt_pk_bf16_f32 v115, v118, v119\n\t"
            "v_exp_f32 v121, v121\n\t"
            "v_exp_f32 v122, v122\n\t"
            "v_mfma_f32_32x32x16_bf16 v[64:79], v[40:43], v[112:115], v[64:79]\n\t"
            "v_exp_f32 v123, v123\n\t"
            "v_cvt_pk_bf16_f32 v120, v120, v121\n\t"
            "v_exp_f32 v124, v124\n\t"
            "v_exp_f32 v125, v125\n\t"
            "v_cvt_pk_bf16_f32 v121, v122, v123\n\t"
            "v_exp_f32 v126, v126\n\t"
            "v_exp_f32 v127, v127\n\t"
            "v_cvt_pk_bf16_f32 v122, v124, v125\n\t"
            "s_nop 0\n\t"
            "v_cvt_pk_bf16_f32 v123, v126, v127\n\t"
            "s_nop 1\n\t"
            "v_mfma_f32_32x32x16_bf16 v[64:79], v[36:39], v[120:123], v[64:79]\n\t"
            "s_mov_b64 exec, %9\n\t"
            "ds_read_b128 v[40:43], %1 offset:14336\n\t"
            "ds_read_b128 v[36:39], %1 offset:14848\n\t"
            "s_mov_b64 exec, -1\n\t"
            "s_cmp_eq_u32 %3, 3\n\t"
            "s_cbranch_scc0 NOH_%=\n\t"
            "s_waitcnt lgkmcnt(0)\n\t"
            "s_barrier\n\t"
            "s_mov_b64 s[62:63], %11\n\t"
            "s_add_u32 s62, s62, 393216\n\t"
            "s_addc_u32 s63, s63, 0\n\t"
            "s_add_u32 m0, %6, 32768\n\t"
            "s_nop 0\n\t"
            "global_load_lds_dwordx4 %13, s[62:63]\n\t"
            "s_mov_b64 s[62:63], %11\n\t"
            "s_add_u32 s62, s62, 458752\n\t"
            "s_addc_u32 s63, s63, 0\n\t"
            "s_add_u32 m0, %6, 40960\n\t"
            "s_nop 0\n\t"
            "global_load_lds_dwordx4 %13, s[62:63]\n\t"
            "NOH_%=:\n\t"
            "s_waitcnt lgkmcnt(2)\n\t"
            "v_mfma_f32_32x32x16_bf16 v[112:127], v[52:55], v[60:63], v[80:95]\n\t"
            "ds_read_b128 v[56:59], %0 offset:16384\n\t"
            "v_exp_f32 v96, v96\n\t"
            "v_exp_f32 v97, v97\n\t"
            "v_exp_f32 v98, v98\n\t"
            "v_exp_f32 v99, v99\n\t"
            "v_cvt_pk_bf16_f32 v96, v96, v97\n\t"
            "v_exp_f32 v100, v100\n\t"
            "v_exp_f32 v101, v101\n\t"
            "v_cvt_pk_bf16_f32 v97, v98, v99\n\t"
            "v_exp_f32 v102, v102\n\t"
            "v_exp_f32 v103, v103\n\t"
            "v_cvt_pk_bf16_f32 v98, v100, v101\n\t"
            "v_exp_f32 v104, v104\n\t"
            "v_cvt_pk_bf16_f32 v99, v102, v103\n\t"
            "v_exp_f32 v105, v105\n\t"
            "v_exp_f32 v106, v106\n\t"
            "v_mfma_f32_32x32x16_bf16 v[64:79], v[48:51], v[96:99], v[64:79]\n\t"
            "v_exp_f32 v107, v107\n\t"
            "v_cvt_pk_bf16_f32 v104, v104, v105\n\t"
            "v_exp_f32 v108, v108\n\t"
            "v_exp_f32 v109, v109\n\t"
            "v_cvt_pk_bf16_f32 v105, v106, v107\n\t"
            "v_exp_f32 v110, v110\n\t"
            "v_exp_f32 v111, v111\n\t"
            "v_cvt_pk_bf16_f32 v106, v108, v109\n\t"
            "s_nop 0\n\t"
            "v_cvt_pk_bf16_f32 v107, v110, v111\n\t"
            "s_nop 1\n\t"
            "v_mfma_f32_32x32x16_bf16 v[64:79], v[44:47], v[104:107], v[64:79]\n\t"
            "s_mov_b64 exec, %9\n\t"
            "ds_read_b128 v[48:51], %1 offset:16384\n\t"
            "ds_read_b128 v[44:47], %1 offset:16896\n\t"
            "s_mov_b64 exec, -1\n\t"
            "s_waitcnt lgkmcnt(2)\n\t"
            "v_mfma_f32_32x32x16_bf16 v[96:111], v[56:59], v[60:63], v[80:95]\n\t"
            "ds_read_b128 v[52:55], %0 offset:18432\n\t"
            "v_exp_f32 v112, v112\n\t"
            "v_exp_f32 v113, v113\n\t"
            "v_exp_f32 v114, v114\n\t"
            "v_exp_f32 v115, v115\n\t"
            "v_cvt_pk_bf16_f32 v112, v112, v113\n\t"
            "v_exp_f32 v116, v116\n\t"
            "v_exp_f32 v117, v117\n\t"
            "v_cvt_pk_bf16_f32 v113, v114, v115\n\t"
            "v_exp_f32 v118, v118\n\t"
            "v_exp_f32 v119, v119\n\t"
            "v_cvt_pk_bf16_f32 v114, v116, v117\n\t"
            "v_exp_f32 v120, v120\n\t"
            "v_cvt_pk_bf16_f32 v115, v118, v119\n\t"
            "v_exp_f32 v121, v121\n\t"
            "v_exp_f32 v122, v122\n\t"
            "v_mfma_f32_32x32x16_bf16 v[64:79], v[40:43], v[112:115], v[64:79]\n\t"
            "v_exp_f32 v123, v123\n\t"
            "v_cvt_pk_bf16_f32 v120, v120, v121\n\t"
            "v_exp_f32 v124, v124\n\t"
            "v_exp_f32 v125, v125\n\t"
            "v_cvt_pk_bf16_f32 v121, v122, v123\n\t"
            "v_exp_f32 v126, v126\n\t"
            "v_exp_f32 v127, v127\n\t"
            "v_cvt_pk_bf16_f32 v122, v124, v125\n\t"
            "s_nop 0\n\t"
            "v_cvt_pk_bf16_f32 v123, v126, v127\n\t"
            "s_nop 1\n\t"
            "v_mfma_f32_32x32x16_bf16 v[64:79], v[36:39], v[120:123], v[64:79]\n\t"
            "s_mov_b64 exec, %9\n\t"
            "ds_read_b128 v[40:43], %1 offset:18432\n\t"
            "ds_read_b128 v[36:39], %1 offset:18944\n\t"
            "s_mov_b64 exec, -1\n\t"
            "s_waitcnt lgkmcnt(2)\n\t"
            "v_mfma_f32_32x32x16_bf16 v[112:127], v[52:55], v[60:63], v[80:95]\n\t"
            "ds_read_b128 v[56:59], %0 offset:20480\n\t"
            "v_exp_f32 v96, v96\n\t"
            "v_exp_f32 v97, v97\n\t"
            "v_exp_f32 v98, v98\n\t"
            "v_exp_f32 v99, v99\n\t"
            "v_cvt_pk_bf16_f32 v96, v96, v97\n\t"
            "v_exp_f32 v100, v100\n\t"
            "v_exp_f32 v101, v101\n\t"
            "v_cvt_pk_bf16_f32 v97, v98, v99\n\t"
            "v_exp_f32 v102, v102\n\t"
            "v_exp_f32 v103, v103\n\t"
            "v_cvt_pk_bf16_f32 v98, v100, v101\n\t"
            "v_exp_f32 v104, v104\n\t"
            "v_cvt_pk_bf16_f32 v99, v102, v103\n\t"
            "v_exp_f32 v105, v105\n\t"
            "v_exp_f32 v106, v106\n\t"
            "v_mfma_f32_32x32x16_bf16 v[64:79], v[48:51], v[96:99], v[64:79]\n\t"
            "v_exp_f32 v107, v107\n\t"
            "v_cvt_pk_bf16_f32 v104, v104, v105\n\t"
            "v_exp_f32 v108, v108\n\t"
            "v_exp_f32 v109, v109\n\t"
            "v_cvt_pk_bf16_f32 v105, v106, v107\n\t"
            "v_exp_f32 v110, v110\n\t"
            "v_exp_f32 v111, v111\n\t"
            "v_cvt_pk_bf16_f32 v106, v108, v109\n\t"
            "s_nop 0\n\t"
            "v_cvt_pk_bf16_f32 v107, v110, v111\n\t"
            "s_nop 1\n\t"
            "v_mfma_f32_32x32x16_bf16 v[64:79], v[44:47], v[104:107], v[64:79]\n\t"
            "s_mov_b64 exec, %9\n\t"
            "ds_read_b128 v[48:51], %1 offset:20480\n\t"
            "ds_read_b128 v[44:47], %1 offset:20992\n\t"
            "s_mov_b64 exec, -1\n\t"
            "s_waitcnt lgkmcnt(2)\n\t"
            "v_mfma_f32_32x32x16_bf16 v[96:111], v[56:59], v[60:63], v[80:95]\n\t"
            "ds_read_b128 v[52:55], %0 offset:22528\n\t"
            "v_exp_f32 v112, v112\n\t"
            "v_exp_f32 v113, v113\n\t"
            "v_exp_f32 v114, v114\n\t"
            "v_exp_f32 v115, v115\n\t"
            "v_cvt_pk_bf16_f32 v112, v112, v113\n\t"
            "v_exp_f32 v116, v116\n\t"
            "v_exp_f32 v117, v117\n\t"
            "v_cvt_pk_bf16_f32 v113, v114, v115\n\t"
            "v_exp_f32 v118, v118\n\t"
            "v_exp_f32 v119, v119\n\t"
            "v_cvt_pk_bf16_f32 v114, v116, v117\n\t"
            "v_exp_f32 v120, v120\n\t"
            "v_cvt_pk_bf16_f32 v115, v118, v119\n\t"
            "v_exp_f32 v121, v121\n\t"
            "v_exp_f32 v122, v122\n\t"
            "v_mfma_f32_32x32x16_bf16 v[64:79], v[40:43], v[112:115], v[64:79]\n\t"
            "v_exp_f32 v123, v123\n\t"
            "v_cvt_pk_bf16_f32 v120, v120, v121\n\t"
            "v_exp_f32 v124, v124\n\t"
            "v_exp_f32 v125, v125\n\t"
            "v_cvt_pk_bf16_f32 v121, v122, v123\n\t"
            "v_exp_f32 v126, v126\n\t"
            "v_exp_f32 v127, v127\n\t"
            "v_cvt_pk_bf16_f32 v122, v124, v125\n\t"
            "s_nop 0\n\t"
            "v_cvt_pk_bf16_f32 v123, v126, v127\n\t"
            "s_nop 1\n\t"
            "v_mfma_f32_32x32x16_bf16 v[64:79], v[36:39], v[120:123], v[64:79]\n\t"
            "s_mov_b64 exec, %9\n\t"
            "ds_read_b128 v[40:43], %1 offset:22528\n\t"
            "ds_read_b128 v[36:39], %1 offset:23040\n\t"
            "s_mov_b64 exec, -1\n\t"
            "s_waitcnt lgkmcnt(2)\n\t"
            "v_mfma_f32_32x32x16_bf16 v[112:127], v[52:55], v[60:63], v[80:95]\n\t"
            "ds_read_b128 v[56:59], %0 offset:24576\n\t"
            "v_exp_f32 v96, v96\n\t"
            "v_exp_f32 v97, v97\n\t"
            "v_exp_f32 v98, v98\n\t"
            "v_exp_f32 v99, v99\n\t"
            "v_cvt_pk_bf16_f32 v96, v96, v97\n\t"
            "v_exp_f32 v100, v100\n\t"
            "v_exp_f32 v101, v101\n\t"
            "v_cvt_pk_bf16_f32 v97, v98, v99\n\t"
            "v_exp_f32 v102, v102\n\t"
            "v_exp_f32 v103, v103\n\t"
            "v_cvt_pk_bf16_f32 v98, v100, v101\n\t"
            "v_exp_f32 v104, v104\n\t"
            "v_cvt_pk_bf16_f32 v99, v102, v103\n\t"
            "v_exp_f32 v105, v105\n\t"
            "v_exp_f32 v106, v106\n\t"
            "v_mfma_f32_32x32x16_bf16 v[64:79], v[48:51], v[96:99], v[64:79]\n\t"
            "v_exp_f32 v107, v107\n\t"
            "v_cvt_pk_bf16_f32 v104, v104, v105\n\t"
            "v_exp_f32 v108, v108\n\t"
            "v_exp_f32 v109, v109\n\t"
            "v_cvt_pk_bf16_f32 v105, v106, v107\n\t"
            "v_exp_f32 v110, v110\n\t"
            "v_exp_f32 v111, v111\n\t"
            "v_cvt_pk_bf16_f32 v106, v108, v109\n\t"
            "s_nop 0\n\t"
            "v_cvt_pk_bf16_f32 v107, v110, v111\n\t"
            "s_nop 1\n\t"
            "v_mfma_f32_32x32x16_bf16 v[64:79], v[44:47], v[104:107], v[64:79]\n\t"
            "s_mov_b64 exec, %9\n\t"
            "ds_read_b128 v[48:51], %1 offset:24576\n\t"
            "ds_read_b128 v[44:47], %1 offset:25088\n\t"
            "s_mov_b64 exec, -1\n\t"
            "s_waitcnt lgkmcnt(2)\n\t"
            "v_mfma_f32_32x32x16_bf16 v[96:111], v[56:59], v[60:63], v[80:95]\n\t"
            "ds_read_b128 v[52:55], %0 offset:26624\n\t"
            "v_exp_f32 v112, v112\n\t"
            "v_exp_f32 v113, v113\n\t"
            "v_exp_f32 v114, v114\n\t"
            "v_exp_f32 v115, v115\n\t"
            "v_cvt_pk_bf16_f32 v112, v112, v113\n\t"
            "v_exp_f32 v116, v116\n\t"
            "v_exp_f32 v117, v117\n\t"
            "v_cvt_pk_bf16_f32 v113, v114, v115\n\t"
            "v_exp_f32 v118, v118\n\t"
            "v_exp_f32 v119, v119\n\t"
            "v_cvt_pk_bf16_f32 v114, v116, v117\n\t"
            "v_exp_f32 v120, v120\n\t"
            "v_cvt_pk_bf16_f32 v115, v118, v119\n\t"
            "v_exp_f32 v121, v121\n\t"
            "v_exp_f32 v122, v122\n\t"
            "v_mfma_f32_32x32x16_bf16 v[64:79], v[40:43], v[112:115], v[64:79]\n\t"
            "v_exp_f32 v123, v123\n\t"
            "v_cvt_pk_bf16_f32 v120, v120, v121\n\t"
            "v_exp_f32 v124, v124\n\t"
            "v_exp_f32 v125, v125\n\t"
            "v_cvt_pk_bf16_f32 v121, v122, v123\n\t"
            "v_exp_f32 v126, v126\n\t"
            "v_exp_f32 v127, v127\n\t"
            "v_cvt_pk_bf16_f32 v122, v124, v125\n\t"
            "s_nop 0\n\t"
            "v_cvt_pk_bf16_f32 v123, v126, v127\n\t"
            "s_nop 1\n\t"
            "v_mfma_f32_32x32x16_bf16 v[64:79], v[36:39], v[120:123], v[64:79]\n\t"
            "s_mov_b64 exec, %9\n\t"
            "ds_read_b128 v[40:43], %1 offset:26624\n\t"
            "ds_read_b128 v[36:39], %1 offset:27136\n\t"
            "s_mov_b64 exec, -1\n\t"
            "s_waitcnt lgkmcnt(2)\n\t"
            "v_mfma_f32_32x32x16_bf16 v[112:127], v[52:55], v[60:63], v[80:95]\n\t"
            "ds_read_b128 v[56:59], %0 offset:28672\n\t"
            "v_exp_f32 v96, v96\n\t"
            "v_exp_f32 v97, v97\n\t"
            "v_exp_f32 v98, v98\n\t"
            "v_exp_f32 v99, v99\n\t"
            "v_cvt_pk_bf16_f32 v96, v96, v97\n\t"
            "v_exp_f32 v100, v100\n\t"
            "v_exp_f32 v101, v101\n\t"
            "v_cvt_pk_bf16_f32 v97, v98, v99\n\t"
            "v_exp_f32 v102, v102\n\t"
            "v_exp_f32 v103, v103\n\t"
            "v_cvt_pk_bf16_f32 v98, v100, v101\n\t"
            "v_exp_f32 v104, v104\n\t"
            "v_cvt_pk_bf16_f32 v99, v102, v103\n\t"
            "v_exp_f32 v105, v105\n\t"
            "v_exp_f32 v106, v106\n\t"
            "v_mfma_f32_32x32x16_bf16 v[64:79], v[48:51], v[96:99], v[64:79]\n\t"
            "v_exp_f32 v107, v107\n\t"
            "v_cvt_pk_bf16_f32 v104, v104, v105\n\t"
            "v_exp_f32 v108, v108\n\t"
            "v_exp_f32 v109, v109\n\t"
            "v_cvt_pk_bf16_f32 v105, v106, v107\n\t"
            "v_exp_f32 v110, v110\n\t"
            "v_exp_f32 v111, v111\n\t"
            "v_cvt_pk_bf16_f32 v106, v108, v109\n\t"
            "s_nop 0\n\t"
            "v_cvt_pk_bf16_f32 v107, v110, v111\n\t"
            "s_nop 1\n\t"
            "v_mfma_f32_32x32x16_bf16 v[64:79], v[44:47], v[104:107], v[64:79]\n\t"
            "s_mov_b64 exec, %9\n\t"
            "ds_read_b128 v[48:51], %1 offset:28672\n\t"
            "ds_read_b128 v[44:47], %1 offset:29184\n\t"
            "s_mov_b64 exec, -1\n\t"
            "s_waitcnt lgkmcnt(2)\n\t"
            "v_mfma_f32_32x32x16_bf16 v[96:111], v[56:59], v[60:63], v[80:95]\n\t"
            "ds_read_b128 v[52:55], %0 offset:30720\n\t"
            "v_exp_f32 v112, v112\n\t"
            "v_exp_f32 v113, v113\n\t"
            "v_exp_f32 v114, v114\n\t"
            "v_exp_f32 v115, v115\n\t"
            "v_cvt_pk_bf16_f32 v112, v112, v113\n\t"
            "v_exp_f32 v116, v116\n\t"
            "v_exp_f32 v117, v117\n\t"
            "v_cvt_pk_bf16_f32 v113, v114, v115\n\t"
            "v_exp_f32 v118, v118\n\t"
            "v_exp_f32 v119, v119\n\t"
            "v_cvt_pk_bf16_f32 v114, v116, v117\n\t"
            "v_exp_f32 v120, v120\n\t"
            "v_cvt_pk_bf16_f32 v115, v118, v119\n\t"
            "v_exp_f32 v121, v121\n\t"
            "v_exp_f32 v122, v122\n\t"
            "v_mfma_f32_32x32x16_bf16 v[64:79], v[40:43], v[112:115], v[64:79]\n\t"
            "v_exp_f32 v123, v123\n\t"
            "v_cvt_pk_bf16_f32 v120, v120, v121\n\t"
            "v_exp_f32 v124, v124\n\t"
            "v_exp_f32 v125, v125\n\t"
            "v_cvt_pk_bf16_f32 v121, v122, v123\n\t"
            "v_exp_f32 v126, v126\n\t"
            "v_exp_f32 v127, v127\n\t"
            "v_cvt_pk_bf16_f32 v122, v124, v125\n\t"
            "s_nop 0\n\t"
            "v_cvt_pk_bf16_f32 v123, v126, v127\n\t"
            "s_nop 1\n\t"
            "v_mfma_f32_32x32x16_bf16 v[64:79], v[36:39], v[120:123], v[64:79]\n\t"
            "s_mov_b64 exec, %9\n\t"
            "ds_read_b128 v[40:43], %1 offset:30720\n\t"
            "ds_read_b128 v[36:39], %1 offset:31232\n\t"
            "s_mov_b64 exec, -1\n\t"
            "s_waitcnt vmcnt(0) lgkmcnt(0)\n\t"
            "s_barrier\n\t"
            "s_cmp_eq_u32 %3, 3\n\t"
            "s_cbranch_scc1 NOB_%=\n\t"
            "s_cmp_eq_u32 %3, 2\n\t"
            "s_cbranch_scc1 WO_%=\n\t"
            "s_mov_b32 m0, %4\n\t"
            "s_add_u32 %4, %4, 1024\n\t"
            "global_load_lds_dwordx4 %12, s[60:61] sc1\n\t"
            "s_add_u32 s60, s60, 1024\n\t"
            "s_addc_u32 s61, s61, 0\n\t"
            "s_mov_b32 m0, %4\n\t"
            "s_add_u32 %4, %4, 1024\n\t"
            "global_load_lds_dwordx4 %12, s[60:61] sc1\n\t"
            "s_add_u32 s60, s60, 1024\n\t"
            "s_addc_u32 s61, s61, 0\n\t"
            "s_mov_b32 m0, %4\n\t"
            "s_add_u32 %4, %4, 1024\n\t"
            "global_load_lds_dwordx4 %12, s[60:61] sc1\n\t"
            "s_add_u32 s60, s60, 1024\n\t"
            "s_addc_u32 s61, s61, 0\n\t"
            "s_mov_b32 m0, %4\n\t"
            "s_add_u32 %4, %4, 1024\n\t"
            "global_load_lds_dwordx4 %12, s[60:61] sc1\n\t"
            "s_add_u32 s60, s60, 29696\n\t"
            "s_addc_u32 s61, s61, 0\n\t"
            "s_add_u32 %4, %4, 28672\n\t"
            "s_cmp_eq_u32 %3, 0\n\t"
            "s_cbranch_scc0 NOB_%=\n\t"
            "s_mov_b64 s[62:63], %11\n\t"
            "s_add_u32 s62, s62, 262144\n\t"
            "s_addc_u32 s63, s63, 0\n\t"
            "s_add_u32 m0, %6, 65536\n\t"
            "s_nop 0\n\t"
            "global_load_lds_dwordx4 %13, s[62:63]\n\t"
            "s_mov_b64 s[62:63], %11\n\t"
            "s_add_u32 s62, s62, 327680\n\t"
            "s_addc_u32 s63, s63, 0\n\t"
            "s_add_u32 m0, %6, 73728\n\t"
            "s_nop 0\n\t"
            "global_load_lds_dwordx4 %13, s[62:63]\n\t"
            "s_branch NOB_%=\n\t"
            "WO_%=:\n\t"
            "s_mov_b64 s[62:63], %11\n\t"
            "s_add_u32 s62, s62, 0\n\t"
            "s_addc_u32 s63, s63, 0\n\t"
            "s_add_u32 m0, %6, 0\n\t"
            "s_nop 0\n\t"
            "global_load_lds_dwordx4 %13, s[62:63]\n\t"
            "s_mov_b64 s[62:63], %11\n\t"
            "s_add_u32 s62, s62, 65536\n\t"
            "s_addc_u32 s63, s63, 0\n\t"
            "s_add_u32 m0, %6, 8192\n\t"
            "s_nop 0\n\t"
            "global_load_lds_dwordx4 %13, s[62:63]\n\t"
            "s_mov_b64 s[62:63], %11\n\t"
            "s_add_u32 s62, s62, 131072\n\t"
            "s_addc_u32 s63, s63, 0\n\t"
            "s_add_u32 m0, %6, 16384\n\t"
            "s_nop 0\n\t"
            "global_load_lds_dwordx4 %13, s[62:63]\n\t"
            "s_mov_b64 s[62:63], %11\n\t"
            "s_add_u32 s62, s62, 196608\n\t"
            "s_addc_u32 s63, s63, 0\n\t"
            "s_add_u32 m0, %6, 24576\n\t"
            "s_nop 0\n\t"
            "global_load_lds_dwordx4 %13, s[62:63]\n\t"
            "NOB_%=:\n\t"
            "v_xor_b32 %0, 0x8000, %0\n\t"
            "v_xor_b32 %1, 0x8000, %1\n\t"
            "s_waitcnt lgkmcnt(2)\n\t"
            "v_mfma_f32_32x32x16_bf16 v[112:127], v[52:55], v[60:63], v[80:95]\n\t"
            "ds_read_b128 v[56:59], %0 offset:0\n\t"
            "v_exp_f32 v96, v96\n\t"
            "v_exp_f32 v97, v97\n\t"
            "v_exp_f32 v98, v98\n\t"
            "v_exp_f32 v99, v99\n\t"
            "v_cvt_pk_bf16_f32 v96, v96, v97\n\t"
            "v_exp_f32 v100, v100\n\t"
            "v_exp_f32 v101, v101\n\t"
            "v_cvt_pk_bf16_f32 v97, v98, v99\n\t"
            "v_exp_f32 v102, v102\n\t"
            "v_exp_f32 v103, v103\n\t"
            "v_cvt_pk_bf16_f32 v98, v100, v101\n\t"
            "v_exp_f32 v104, v104\n\t"
            "v_cvt_pk_bf16_f32 v99, v102, v103\n\t"
            "v_exp_f32 v105, v105\n\t"
            "v_exp_f32 v106, v106\n\t"
            "v_mfma_f32_32x32x16_bf16 v[64:79], v[48:51], v[96:99], v[64:79]\n\t"
            "v_exp_f32 v107, v107\n\t"
            "v_cvt_pk_bf16_f32 v104, v104, v105\n\t"
            "v_exp_f32 v108, v108\n\t"
            "v_exp_f32 v109, v109\n\t"
            "v_cvt_pk_bf16_f32 v105, v106, v107\n\t"
            "v_exp_f32 v110, v110\n\t"
            "v_exp_f32 v111, v111\n\t"
            "v_cvt_pk_bf16_f32 v106, v108, v109\n\t"
            "s_nop 0\n\t"
            "v_cvt_pk_bf16_f32 v107, v110, v111\n\t"
            "s_nop 1\n\t"
            "v_mfma_f32_32x32x16_bf16 v[64:79], v[44:47], v[104:107], v[64:79]\n\t"
            "s_mov_b64 exec, %9\n\t"
            "ds_read_b128 v[48:51], %1 offset:0\n\t"
            "ds_read_b128 v[44:47], %1 offset:512\n\t"
            "s_mov_b64 exec, -1\n\t"
            "s_waitcnt lgkmcnt(2)\n\t"
            "v_mfma_f32_32x32x16_bf16 v[96:111], v[56:59], v[60:63], v[80:95]\n\t"
            "ds_read_b128 v[52:55], %0 offset:2048\n\t"
            "v_exp_f32 v112, v112\n\t"
            "v_exp_f32 v113, v113\n\t"
            "v_exp_f32 v114, v114\n\t"
            "v_exp_f32 v115, v115\n\t"
            "v_cvt_pk_bf16_f32 v112, v112, v113\n\t"
            "v_exp_f32 v116, v116\n\t"
            "v_exp_f32 v117, v117\n\t"
            "v_cvt_pk_bf16_f32 v113, v114, v115\n\t"
            "v_exp_f32 v118, v118\n\t"
            "v_exp_f32 v119, v119\n\t"
            "v_cvt_pk_bf16_f32 v114, v116, v117\n\t"
            "v_exp_f32 v120, v120\n\t"
            "v_cvt_pk_bf16_f32 v115, v118, v119\n\t"
            "v_exp_f32 v121, v121\n\t"
            "v_exp_f32 v122, v122\n\t"
            "v_mfma_f32_32x32x16_bf16 v[64:79], v[40:43], v[112:115], v[64:79]\n\t"
            "v_exp_f32 v123, v123\n\t"
            "v_cvt_pk_bf16_f32 v120, v120, v121\n\t"
            "v_exp_f32 v124, v124\n\t"
            "v_exp_f32 v125, v125\n\t"
            "v_cvt_pk_bf16_f32 v121, v122, v123\n\t"
            "v_exp_f32 v126, v126\n\t"
            "v_exp_f32 v127, v127\n\t"
            "v_cvt_pk_bf16_f32 v122, v124, v125\n\t"
            "s_nop 0\n\t"
            "v_cvt_pk_bf16_f32 v123, v126, v127\n\t"
            "s_nop 1\n\t"
            "v_mfma_f32_32x32x16_bf16 v[64:79], v[36:39], v[120:123], v[64:79]\n\t"
            "s_mov_b64 exec, %9\n\t"
            "ds_read_b128 v[40:43], %1 offset:2048\n\t"
            "ds_read_b128 v[36:39], %1 offset:2560\n\t"
            "s_mov_b64 exec, -1\n\t"
            "s_add_u32 %3, %3, 1\n\t"
            "s_cmp_lg_u32 %3, 4\n\t"
            "s_cbranch_scc1 LOOP_%=\n\t"
            "s_waitcnt lgkmcnt(0)\n\t"
            "s_nop 15\n\t"
            "s_mov_b32 m0, %2\n\t"
            : "+v"(kp), "+v"(vp), "=&s"(keep), "=&s"(ctr), "+s"(ldskv), "=&{v[64:79]}"(o), "+s"(ldswo)
            : "v"(negm), "{v[60:63]}"(qp), "s"(0x0000ffff0000ffffull), "s"(kvg + 65536), "s"(P.bwo), "v"(kvoff), "v"(wooff), "v"((tidk & 31) == 16 ? 0x3f803f80u : 0u)
            : "memory", "scc", "s60", "s61", "s62", "s63",
              "v36", "v37", "v38", "v39", "v40", "v41", "v42", "v43", "v44", "v45", "v46", "v47", "v48", "v49", "v50", "v51",
              "v52", "v53", "v54", "v55", "v56", "v57", "v58", "v59",
              "v80", "v81", "v82", "v83", "v84", "v85", "v86", "v87", "v88", "v89", "v90", "v91", "v92", "v93", "v94", "v95",
              "v96", "v97", "v98", "v99", "v100", "v101", "v102", "v103", "v104", "v105", "v106", "v107", "v108", "v109", "v110", "v111",
              "v112", "v113", "v114", "v115", "v116", "v117", "v118", "v119", "v120", "v121", "v122", "v123", "v124", "v125", "v126", "v127");
      }
      {
      LANE_VARS();
      bool bad = !(o[8] > 0.f && o[8] < 1.0e30f);
#pragma unroll
      for (int j = 0; j < 8; j++) bad |= !(fabsf(o[j]) < 3.0e38f);
      if (__any(bad)) {
        float m = -3.0e38f;
#pragma unroll
        for (int i = 0; i < 16; i++) o[i] = 0.f;
#pragma unroll 1
        for (int t = 0; t < 64; t++) {
          const bf16x8 kf = __builtin_bit_cast(bf16x8, ld16_sc1(kvg, t * 2048 + lane * 16));
          u32x4 v0 = ld16_sc1(kvg, t * 2048 + 1024 + (h * 16 + (r & 15)) * 16);
          u32x4 v1 = ld16_sc1(kvg, t * 2048 + 1536 + (h * 16 + (r & 15)) * 16);
          if (r >= 16) { v0 = u32x4{0x3f803f80u, 0x3f803f80u, 0x3f803f80u, 0x3f803f80u}; v1 = v0; }
          const f32x16 z = {0};
          const f32x16 s = MFMA(kf, qf, z);
          float mt = s[0];
#pragma unroll
          for (int i = 1; i < 16; i++) mt = fmaxf(mt, s[i]);
          mt = fmaxf(mt, __shfl_xor(mt, 32));
          const float mn = fmaxf(m, mt);
          const float al = __builtin_amdgcn_exp2f(m - mn);
          m = mn;
#pragma unroll
          for (int i = 0; i < 16; i++) o[i] *= al;
          u32x4 p0, p1;
#pragma unroll
          for (int i = 0; i < 4; i++) {
            p0[i] = pk2(__builtin_amdgcn_exp2f(s[2 * i] - m), __builtin_amdgcn_exp2f(s[2 * i + 1] - m));
            p1[i] = pk2(__builtin_amdgcn_exp2f(s[8 + 2 * i] - m), __builtin_amdgcn_exp2f(s[8 + 2 * i + 1] - m));
          }
          o = MFMA(__builtin_bit_cast(bf16x8, v0), __builtin_bit_cast(bf16x8, p0), o);
          o = MFMA(__builtin_bit_cast(bf16x8, v1), __builtin_bit_cast(bf16x8, p1), o);
        }
      }
      const float il = 1.0f / o[8];
      u32x4 ov = {pk2(o[0] * il, o[1] * il), pk2(o[2] * il, o[3] * il), pk2(o[4] * il, o[5] * il), pk2(o[6] * il, o[7] * il)};
      st16_sc1(P.ws + WS_OB, ((rt * 2 + qt) * 32 + hg * 4 + head) * 1024 + lane * 16, ov);
      myflag = (rt * 2 + qt) * 32 + hg * 4 + head;
      }
    }
    wo = P.bwo; oimg = P.ws + WS_OB; oflag = P.ws + WS_BFLAG; outcol = 512;
  }

  {
  LANE_VARS();
  {
    const int qt = w >> 2, kq = w & 3;
    const unsigned trsw = ((q4 >> 1) & 1) << 2;
    const unsigned qbase = kq < 2 ? kq * 16384 : (kq == 2 ? 65536 : 32768);
    const unsigned ta = qbase + (4 * (g >> 1) + q4) * 128 + 8 * (p4 & 1) + (((2 * (g & 1) + (p4 >> 1)) ^ trsw) << 4);
    bf16x8 wf0[8], wf1[8];
    u32x4 of[8];
    auto rd_wo = [&]() {
#pragma unroll
      for (int i = 0; i < 8; i++) {
        wf0[i] = lds_frag_tr(sm, ta + i * 2048, ta + i * 2048 + 1024);
        wf1[i] = lds_frag_tr(sm, (ta ^ 64) + i * 2048, (ta ^ 64) + i * 2048 + 1024);
      }
    };
    auto get_o = [&]() {
      poll_flags<8>(oflag + ((rt * 2 + qt) * 32 + kq * 8) * 128, tag, lane);
#pragma unroll
      for (int i = 0; i < 8; i++) of[i] = ld16_sc1(oimg, ((rt * 2 + qt) * 32 + kq * 8 + i) * 1024 + lane * 16);
    };
    if (roleA) {
      WAIT_VM(0);
      if (lane == 0) __hip_atomic_store((AS1 u64*)oflag + myflag * 16, tag, RLX_AGENT);
      bar();
      const unsigned vo = (8 * w + prow) * 1024 + hg * 128 + ((pch ^ (((prow >> 1) & 1) << 2)) << 4);
#pragma unroll
      for (int i = 0; i < 4; i++) glds16(wo + (4 + i) * 64 * 1024, vo, ldsb + (i < 2 ? 65536 : 16384) + i * 8192 + w * 1024);
      get_o();
      WAIT_VM(0);
      bar();
      rd_wo();
#pragma unroll
      for (int i = 0; i < 8; i++) asm volatile("" : "+v"(wf0[i]), "+v"(wf1[i]));
      bar();
    } else {
      rd_wo();
#pragma unroll
      for (int i = 0; i < 8; i++) asm volatile("" : "+v"(wf0[i]), "+v"(wf1[i]));
      bar();
      WAIT_VM(0);
      if (lane == 0) __hip_atomic_store((AS1 u64*)oflag + myflag * 16, tag, RLX_AGENT);
      if (bid == 255 && w == 1) {
        u64 t0 = __builtin_amdgcn_s_memrealtime();
        while (true) {
          bool ok = true;
#pragma unroll
          for (int i = 0; i < 8; i++) ok &= __hip_atomic_load((AS1 u64*)(P.ws + WS_START + (i * 64 + lane) * 128), RLX_AGENT) == tag;
          if (__all(ok)) break;
          if (__builtin_amdgcn_s_memrealtime() - t0 > SPIN_TICKS) break;
          __builtin_amdgcn_s_sleep(2);
        }
        if (lane == 0) __hip_atomic_store((AS1 u64*)(P.ws + WS_EPOCH), tag, RLX_AGENT);
      }
      get_o();
    }
    f32x16 c0 = {0}, c1 = {0};
#pragma unroll
    for (int i = 0; i < 8; i++) {
      c0 = MFMA(wf0[i], __builtin_bit_cast(bf16x8, of[i]), c0);
      c1 = MFMA(wf1[i], __builtin_bit_cast(bf16x8, of[i]), c1);
    }
    AS3 f32x4* ex = (AS3 f32x4*)sm;
    f32x4 lo, hi;
#pragma unroll
    for (int d = 0; d < 4; d++) {
      const f32x16& c = d < 2 ? c0 : c1;
      const f32x4 x0 = {c[8 * (d & 1) + 0], c[8 * (d & 1) + 1], c[8 * (d & 1) + 2], c[8 * (d & 1) + 3]};
      const f32x4 x1 = {c[8 * (d & 1) + 4], c[8 * (d & 1) + 5], c[8 * (d & 1) + 6], c[8 * (d & 1) + 7]};
      if (d == kq) { lo = x0; hi = x1; }
      else {
        const int sl = ((qt * 4 + d) * 3 + ((kq - d - 1) & 3)) * 128;
        ex[sl + lane] = x0;
        ex[sl + 64 + lane] = x1;
      }
    }
    bar();
#pragma unroll
    for (int s = 0; s < 3; s++) {
      lo += ex[(w * 3 + s) * 128 + lane];
      hi += ex[(w * 3 + s) * 128 + 64 + lane];
    }
    const unsigned a0 = pk2(lo[0], lo[1]), a1 = pk2(lo[2], lo[3]), b0 = pk2(hi[0], hi[1]), b1 = pk2(hi[2], hi[3]);
    const auto x0 = __builtin_amdgcn_permlane32_swap(a0, b0, false, false);
    const auto x1 = __builtin_amdgcn_permlane32_swap(a1, b1, false, false);
    const u32x4 ov = {x0[0], x1[0], x0[1], x1[1]};
    char* dst = P.out + (size_t)(rt * 64 + qt * 32 + r) * 2048 + (outcol + hg * 64 + kq * 16 + h * 8) * 2;
    *(u32x4*)dst = ov;
  }
  }
}

extern "C" void kernel_launch(void* const* d_in, const int* in_sizes, int n_in,
                              void* d_out, int out_size, void* d_ws, size_t ws_size,
                              hipStream_t stream) {
  Params P;
  P.ax = (const char*)d_in[0]; P.awqkv = (const char*)d_in[1]; P.awo = (const char*)d_in[2];
  P.bx = (const char*)d_in[3]; P.bwq = (const char*)d_in[4]; P.bwk = (const char*)d_in[5];
  P.bwv = (const char*)d_in[6]; P.bwo = (const char*)d_in[7];
  P.out = (char*)d_out; P.ws = (char*)d_ws;
  fused_kernel<<<512, 512, 0, stream>>>(P);
}
```
